```python
import torch

def _softmax(x, axis=-1):
    return torch.softmax(x, dim=axis)
def _gelu(x):
    return torch.nn.functional.gelu(x, approximate="tanh")
def _silu(x):
    return torch.nn.functional.silu(x)
def _sigmoid(x):
    return torch.sigmoid(x)
def _layernorm(x, axis=-1, eps=1e-5):
    m = x.mean(dim=axis, keepdim=True)
    v = x.var(dim=axis, keepdim=True, unbiased=False)
    return (x - m) / torch.sqrt(v + eps)
def _scatter_add(tgt, idx, src, axis=0):
    return tgt.clone().index_add_(axis, idx, src)
def _segment_sum(src, seg, n):
    out = torch.zeros((n,) + src.shape[1:], dtype=src.dtype)
    return out.index_add_(0, seg, src)
def _pad(x, pw):
    flat = tuple(v for pair in reversed(pw) for v in pair)
    return torch.nn.functional.pad(x, flat)

kernel_name = 'standalone__linear_kernel_performer__prompt'

def setup_inputs():

    _w = (lambda a: (lambda t: t.to(torch.bfloat16) if t.is_floating_point() else t)(torch.as_tensor(a)))
    B, H, S, D = 1, 32, 2048, 192
    M = D
    return {
        "q": _w((lambda *s: torch.randn(*s, dtype=torch.bfloat16) * 0.05)(B, H, S, D)),
        "k": _w((lambda *s: torch.randn(*s, dtype=torch.bfloat16) * 1.6)(B, H, S, D)),
        "v": _w((lambda *s: torch.randn(*s, dtype=torch.bfloat16) * 1.6)(B, H, S, D)),
        "proj": _w((lambda *s: torch.randn(*s, dtype=torch.bfloat16) * 1.6)(M, D)),
    }

def reference(q, k, v, proj):
    B, H, S, D = q.shape
    M = proj.shape[0]
    scale = D ** -0.25
    qs = q * scale
    ks = k * scale
    qw = torch.einsum("bhsd,md->bhsm", qs, proj)
    kw = torch.einsum("bhsd,md->bhsm", ks, proj)
    qn = torch.einsum("bhsd,bhsd->bhs", qs, qs)[..., None] * 0.5
    kn = torch.einsum("bhsd,bhsd->bhs", ks, ks)[..., None] * 0.5
    inv_sqrt_m = M ** -0.5
    phi_q = torch.exp(qw - qn) * inv_sqrt_m
    phi_k = torch.exp(kw - kn) * inv_sqrt_m
    kv = torch.einsum("bhsm,bhsd->bhmd", phi_k, v)
    ksum = torch.einsum("bhsm->bhm", phi_k)
    num = torch.einsum("bhsm,bhmd->bhsd", phi_q, kv)
    den = torch.einsum("bhsm,bhm->bhs", phi_q, ksum)[..., None] + 1e-6
    return num / den
```

```cpp
#include <hip/hip_runtime.h>
#include <stdint.h>

#define KV_POL 0
#define Q_POL 0
#define OUT_POL 16

typedef __bf16 bf16x8 __attribute__((ext_vector_type(8)));
typedef __bf16 bf16x2 __attribute__((ext_vector_type(2)));
typedef float f32x2 __attribute__((ext_vector_type(2)));
typedef float f32x4 __attribute__((ext_vector_type(4)));
typedef float f32x16 __attribute__((ext_vector_type(16)));
typedef unsigned u32x2 __attribute__((ext_vector_type(2)));
typedef unsigned u32x4 __attribute__((ext_vector_type(4)));
typedef short s16x4 __attribute__((ext_vector_type(4)));
typedef short s16x8 __attribute__((ext_vector_type(8)));
typedef unsigned long long u64;
#define LAS __attribute__((address_space(3)))
#define GAS __attribute__((address_space(1)))
#define DI __device__ __forceinline__

constexpr int NH = 32, SEQ = 2048, ROWB = 384;
constexpr int TILE = 12288;
constexpr int SLOT = 24576;
constexpr int K0 = 0, K1 = SLOT, K2 = 2 * SLOT, V0 = 3 * SLOT, V1 = 4 * SLOT, V2 = 5 * SLOT, AUXR = 6 * SLOT;
constexpr int BIAS_OFF = AUXR;
constexpr int KSS_OFF = AUXR + 512;
constexpr int KSI_OFF = AUXR + 1280;
constexpr int CNT_OFF = AUXR + 1664;
constexpr int LDS_BYTES = AUXR + 2048 + 4096;

constexpr size_t WS_EPOCH = 0;
constexpr size_t WS_FLAG = 8192;
constexpr size_t WS_KSUM = 131072;
constexpr size_t WS_PART = 1048576;
constexpr size_t WS_TS = 16777216;

constexpr float SCALE = 0.2686424829558855f;
constexpr float LOG2E = 1.4426950408889634f;
constexpr float C1 = SCALE * LOG2E;
constexpr float LOG2C = -3.7924812503605780f;

template <int POL> DI void dma16(unsigned m0v, unsigned voff, __amdgpu_buffer_rsrc_t srd, unsigned soff) {
  if constexpr (POL == 0)
    asm volatile("s_mov_b32 m0, %0\n\ts_nop 0\n\tbuffer_load_dwordx4 %1, %2, %3 offen lds" ::"s"(m0v), "v"(voff), "s"(srd), "s"(soff) : "memory");
  else
    asm volatile("s_mov_b32 m0, %0\n\ts_nop 0\n\tbuffer_load_dwordx4 %1, %2, %3 offen nt lds" ::"s"(m0v), "v"(voff), "s"(srd), "s"(soff) : "memory");
}
template <int N> DI void wait_vm() { asm volatile("s_waitcnt vmcnt(%0)" ::"n"(N) : "memory"); }
DI void wait_lgkm0() { asm volatile("s_waitcnt lgkmcnt(0)" ::: "memory"); }
DI void bar0() { asm volatile("s_waitcnt lgkmcnt(0)\n\ts_barrier" ::: "memory"); }
DI unsigned pk2(float a, float b) { f32x2 t = {a, b}; return __builtin_bit_cast(unsigned, __builtin_convertvector(t, bf16x2)); }
DI f32x16 mfma(bf16x8 a, bf16x8 b, f32x16 c) { return __builtin_amdgcn_mfma_f32_32x32x16_bf16(a, b, c, 0, 0, 0); }
DI u32x4 lds_rd16(unsigned a) { return *(const LAS u32x4*)a; }
DI void lds_wr16(unsigned a, u32x4 v) { *(LAS u32x4*)a = v; }
DI bf16x8 asbf(u32x4 v) { return __builtin_bit_cast(bf16x8, v); }
DI s16x4 lds_tr(unsigned a) { return __builtin_amdgcn_ds_read_tr16_b64_v4i16((LAS s16x4*)a); }
DI float dot2(unsigned a, float c) { bf16x2 t = __builtin_bit_cast(bf16x2, a); return __builtin_amdgcn_fdot2_f32_bf16(t, t, c, false); }
DI float sumsq8(u32x4 v, float c) { return dot2(v[3], dot2(v[2], dot2(v[1], dot2(v[0], c)))); }
template <int CTRL> DI float dpp_add(float x) {
  return x + __builtin_bit_cast(float, __builtin_amdgcn_update_dpp(0, __builtin_bit_cast(int, x), CTRL, 0xf, 0xf, true));
}
DI float swap32_add(float x) {
  auto r = __builtin_amdgcn_permlane32_swap(__float_as_uint(x), __float_as_uint(x), false, false);
  return __uint_as_float(r[0]) + __uint_as_float(r[1]);
}
DI float bflo(unsigned x) { return __uint_as_float(x << 16); }
DI float bfhi(unsigned x) { return __uint_as_float(x & 0xffff0000u); }

#define XEXP(x) __builtin_amdgcn_exp2f(x)
#define SB() __builtin_amdgcn_sched_barrier(0)
DI float pin(float x) { asm volatile("" : "+v"(x)); return x; }

#define TS(i) do {} while (0)
#define POLL_AFTER 3

template <int POL> DI void issue_k(unsigned lds, const unsigned (&vk)[6], __amdgpu_buffer_rsrc_t srd, unsigned soff) {
#pragma unroll
  for (int i = 0; i < 12; ++i) dma16<POL>(lds + i * 1024, vk[i % 6], srd, soff + (i / 6) * 6144);
}
template <int POL> DI void issue_v(unsigned lds, const unsigned (&vv)[3], __amdgpu_buffer_rsrc_t srd, unsigned soff) {
#pragma unroll
  for (int i = 0; i < 12; ++i) dma16<POL>(lds + i * 1024, vv[i % 3], srd, soff + (i / 3) * 3072);
}
DI void k_norms(unsigned tile, unsigned biasaddr, int lane) {
  const unsigned a = tile + (lane >> 3) * ROWB + (lane & 7) * 16;
  u32x4 x[12];
#pragma unroll
  for (int i = 0; i < 12; ++i) x[i] = lds_rd16(a + (i / 3) * 8 * ROWB + (i % 3) * 128);
  float t[12];
#pragma unroll
  for (int i = 0; i < 12; ++i) t[i] = dot2(x[i][0], 0.f);
#pragma unroll
  for (int j = 1; j < 4; ++j)
#pragma unroll
    for (int i = 0; i < 12; ++i) t[i] = dot2(x[i][j], t[i]);
  float s[4];
#pragma unroll
  for (int p = 0; p < 4; ++p) s[p] = (t[3 * p] + t[3 * p + 1]) + t[3 * p + 2];
#pragma unroll
  for (int p = 0; p < 4; ++p) s[p] = dpp_add<0xB1>(s[p]);
#pragma unroll
  for (int p = 0; p < 4; ++p) s[p] = dpp_add<0x4E>(s[p]);
#pragma unroll
  for (int p = 0; p < 4; ++p) s[p] = pin(dpp_add<0x141>(s[p]));
  const int p = lane & 3;
  float sel = p == 0 ? s[0] : p == 1 ? s[1] : p == 2 ? s[2] : s[3];
  sel = sel * (-0.5f * SCALE) + LOG2C / C1;
  if ((lane & 7) < 4) *(LAS float*)(biasaddr + (8 * p + (lane >> 3)) * 4) = sel;
}
DI float q_bias(const u32x4 (&Q)[12]) {
  float t[12];
#pragma unroll
  for (int i = 0; i < 12; ++i) t[i] = dot2(Q[i][0], 0.f);
#pragma unroll
  for (int j = 1; j < 4; ++j)
#pragma unroll
    for (int i = 0; i < 12; ++i) t[i] = dot2(Q[i][j], t[i]);
  const float s = ((t[0] + t[1]) + (t[2] + t[3])) + ((t[4] + t[5]) + (t[6] + t[7])) + ((t[8] + t[9]) + (t[10] + t[11]));
  return pin(swap32_add(s) * (-0.5f * SCALE * C1) + LOG2C);
}

DI bf16x8 v_frag(unsigned vE, unsigned vO, int i) {
  const int t = i / 6, n = i % 6;
  const unsigned base = ((n & 1) ? vO : vE) + t * 16 * ROWB + 64 * n;
  s16x4 lo = lds_tr(base), hi = lds_tr(base + 8 * ROWB);
  return __builtin_bit_cast(bf16x8, __builtin_shufflevector(lo, hi, 0, 1, 2, 3, 4, 5, 6, 7));
}
#define NTAIL 3
#define VGAP 3
#define KPF 3
struct P1 {
  f32x16 acc2[6];
  bf16x8 vt[NTAIL];
  u32x4 pa1;
  float ksum;
};
template <bool G1, bool EX, bool TAIL>
DI void p1_round(P1& s, f32x16& cur, const f32x16& prev, const bf16x8 (&P)[12], unsigned kb, unsigned biasaddr, unsigned vE, unsigned vO) {
  constexpr int NIN = 12 - NTAIL;
  constexpr int VOFF = 12 + NIN - 12 - VGAP;
  bf16x8 kf[12], vf[12];
  if constexpr (G1) {
#pragma unroll
    for (int t = 0; t < 1; ++t) kf[t] = asbf(lds_rd16((kb ^ (32 * (t & 3))) + 128 * (t >> 2)));
#pragma unroll
    for (int g = 0; g < 4; ++g) {
      f32x4 b = *(const LAS f32x4*)(biasaddr + g * 32);
      cur[4 * g] = b[0]; cur[4 * g + 1] = b[1]; cur[4 * g + 2] = b[2]; cur[4 * g + 3] = b[3];
    }
#pragma unroll
    for (int t = 1; t < KPF; ++t) kf[t] = asbf(lds_rd16((kb ^ (32 * (t & 3))) + 128 * (t >> 2)));
  }
  SB();
  if constexpr (TAIL) {
#pragma unroll
    for (int i = 0; i < NTAIL; ++i) { s.acc2[6 - NTAIL + i] = mfma(asbf(s.pa1), s.vt[i], s.acc2[6 - NTAIL + i]); SB(); }
  }
  float e[16];
  u32x4 pa0, pa1;
#define EXP1(j) do { e[j] = XEXP(prev[j] * C1); s.ksum += e[j]; \
    if ((j) & 1) { const unsigned w_ = pk2(e[(j) - 1], e[j]); if ((j) < 8) pa0[((j) >> 1) & 3] = w_; else pa1[((j) >> 1) & 3] = w_; } } while (0)
#pragma unroll
  for (int t = 0; t < 12; ++t) {
    if constexpr (G1) {
      cur = mfma(kf[t], P[t], cur);
      if (t + KPF < 12) kf[t + KPF] = asbf(lds_rd16((kb ^ (32 * ((t + KPF) & 3))) + 128 * ((t + KPF) >> 2)));
    }
    if constexpr (EX) {
      if (t >= VOFF) { if (t - VOFF < NIN) vf[t - VOFF] = v_frag(vE, vO, t - VOFF); else s.vt[t - VOFF - NIN] = v_frag(vE, vO, t - VOFF); }
      EXP1(t);
    }
    SB();
  }
  if constexpr (EX) {
#pragma unroll
    for (int i = 0; i < NIN; ++i) {
      s.acc2[i % 6] = mfma(asbf(i < 6 ? pa0 : pa1), vf[i], s.acc2[i % 6]);
      if (12 + i - VOFF < 12) { const int j = 12 + i - VOFF; if (j < NIN) vf[j] = v_frag(vE, vO, j); else s.vt[j - NIN] = v_frag(vE, vO, j); }
      if (i < 4) EXP1(12 + i);
      SB();
    }
    s.pa1 = pa1;
  }
#undef EXP1
}

__global__ __launch_bounds__(512, 2) void performer_kernel(const char* __restrict__ q, const char* __restrict__ k, const char* __restrict__ v,
                                                           const char* __restrict__ proj, char* __restrict__ out, char* ws) {
  extern __shared__ __attribute__((aligned(16))) char smem[];
  const unsigned L0 = (unsigned)(uintptr_t)(LAS char*)smem;
  const int tid = threadIdx.x, lane = tid & 63;
  const int wave = __builtin_amdgcn_readfirstlane(tid >> 6);
  const int bid = blockIdx.x;
  const int head = (bid & 7) + 8 * (bid >> 6), c = (bid >> 3) & 7;
  const int sq = c >> 1, mh = c & 1;
  const int l31 = lane & 31, hh = lane >> 5;
  const int sg = wave >> 2, mt = wave & 3;
#define bar() bar0()
  TS(0);

  u32x4 Q[12];
  u64 tag;
  const char* ep = ws + WS_EPOCH + head * 256;
  float biasq;
  const unsigned swz = l31 * ROWB + ((hh ^ ((l31 >> 1) & 7)) << 4);

  if (mt == 3) {
    const __amdgpu_buffer_rsrc_t srdK = __builtin_amdgcn_make_buffer_rsrc((void*)(k + ((size_t)head * SEQ + sq * 512) * ROWB), 0, 512 * ROWB, 0x00020000);
    const __amdgpu_buffer_rsrc_t srdV = __builtin_amdgcn_make_buffer_rsrc((void*)(v + ((size_t)head * SEQ + sq * 512) * ROWB), 0, 512 * ROWB, 0x00020000);
    const __amdgpu_buffer_rsrc_t srdP = __builtin_amdgcn_make_buffer_rsrc((void*)proj, 0, 192 * ROWB, 0x00020000);
    const __amdgpu_buffer_rsrc_t srdQ = __builtin_amdgcn_make_buffer_rsrc((void*)(q + ((size_t)head * SEQ + c * 256) * ROWB), 0, 256 * ROWB, 0x00020000);
    unsigned vk[6], vv[3];
#pragma unroll
    for (int b = 0; b < 6; ++b) {
      const int pos = b * 64 + lane, row = pos / 24, cs = pos % 24;
      vk[b] = row * ROWB + (((cs & ~7) | ((cs & 7) ^ ((row >> 1) & 7))) << 4);
      if (b < 3) vv[b] = row * ROWB + ((cs ^ (((row >> 1) & 1) << 2)) << 4);
    }
    const unsigned lg = L0 + sg * TILE, bl = L0 + BIAS_OFF + sg * 128;
    const unsigned so = sg * TILE;
    issue_k<KV_POL>(lg + K0, vk, srdK, so); issue_k<KV_POL>(lg + K1, vk, srdK, so + SLOT); issue_v<KV_POL>(lg + V0, vv, srdV, so);
    asm volatile("s_load_dwordx2 %0, %1, 0x0\n\ts_waitcnt lgkmcnt(0)" : "=s"(tag) : "s"(ep) : "memory");
    if (wave == 3 && lane == 0) *(LAS unsigned*)(L0 + CNT_OFF) = 0u;
    wait_vm<24>(); k_norms(lg + K0, bl, lane); bar();
    unsigned s1 = SLOT, s2 = 2 * SLOT;
#pragma unroll 1
    for (int r = 0; r < 6; ++r) {
      issue_k<KV_POL>(lg + K0 + s2, vk, srdK, so + (r + 2) * SLOT); issue_v<KV_POL>(lg + V0 + s1, vv, srdV, so + (r + 1) * SLOT);
      wait_vm<36>(); k_norms(lg + K0 + s1, bl + ((r + 1) & 1) * 256, lane); wait_vm<24>(); bar();
      const unsigned t = s1; s1 = s2; s2 = (t == 0 ? 2 * SLOT : t - SLOT);
    }
    const int c0 = sg, c1 = 2 + sg, c2 = 4 + sg;
    issue_v<KV_POL>(lg + V1, vv, srdV, so + 7 * SLOT);
    issue_k<Q_POL>(lg + K2, vk, srdQ, (c0 + (c0 >= 3)) * TILE);
    wait_vm<36>(); k_norms(lg + K1, bl + 256, lane); wait_vm<24>(); bar();
    issue_k<Q_POL>(lg + K0, vk, srdQ, (c1 + (c1 >= 3)) * TILE);
    issue_k<Q_POL>(lg + V2, vk, srdQ, (c2 + (c2 >= 3)) * TILE);
    {
      const unsigned qoff = (wave * 32 + l31) * ROWB + hh * 16;
#pragma unroll
      for (int t = 0; t < 12; ++t) Q[t] = __builtin_amdgcn_raw_buffer_load_b128(srdQ, qoff + t * 32, 0, Q_POL);
    }
    wait_vm<48>(); bar();
    issue_k<0>(lg + K1, vk, srdP, sg * TILE);
    issue_k<0>(lg + V0, vk, srdP, (2 + sg) * TILE);
    wait_vm<24>(); bar();
    issue_k<0>(lg + V1, vk, srdP, (4 + sg) * TILE);
    biasq = q_bias(Q);
    bar();
    wait_vm<0>();
  } else {
    const int u = 3 * mh + mt, ci = sg * 3 + mt;
    bf16x8 P[12];
    {
      const char* pp = proj + (32 * u + l31) * ROWB + hh * 16;
#pragma unroll
      for (int t = 0; t < 12; ++t) P[t] = asbf(*(const GAS u32x4*)(pp + t * 32));
    }
    asm volatile("s_load_dwordx2 %0, %1, 0x0\n\ts_waitcnt lgkmcnt(0)" : "=s"(tag) : "s"(ep) : "memory");
    P1 s;
#pragma unroll
    for (int n = 0; n < 6; ++n)
#pragma unroll
      for (int i = 0; i < 16; ++i) s.acc2[n][i] = 0.f;
    s.ksum = 0.f;
    const unsigned kb0 = L0 + K0 + sg * TILE + swz;
    const int qq = (lane & 15) >> 2, pp4 = lane & 3, x = (qq >> 1) & 1;
    const unsigned vb0 = L0 + V0 + sg * TILE + (4 * hh + qq) * ROWB + 16 * (2 * ((lane >> 4) & 1) + (pp4 >> 1)) + 8 * (pp4 & 1);
    const unsigned vE0 = vb0 + 64 * x, vO0 = vb0 - 64 * x;
    const unsigned bb0 = L0 + BIAS_OFF + sg * 128 + hh * 16;
    if (sg == 0) __builtin_amdgcn_s_setprio(1);
#pragma unroll
    for (int t = 0; t < 12; ++t) asm volatile("" ::"v"(P[t]));
    f32x16 accA, accB;
    bar();
    TS(1);
    p1_round<true, false, false>(s, accA, accA, P, kb0, bb0, vE0, vO0);
    bar();
    p1_round<true, true, false>(s, accB, accA, P, kb0 + SLOT, bb0 + 256, vE0, vO0);
    bar();
    unsigned ks = 2 * SLOT, vs = SLOT;
#pragma unroll 1
    for (int r = 2; r < 8; r += 2) {
      p1_round<true, true, true>(s, accA, accB, P, kb0 + ks, bb0, vE0 + vs, vO0 + vs);
      bar();
      vs = ks; ks = (ks == 2 * SLOT ? 0 : ks + SLOT);
      p1_round<true, true, true>(s, accB, accA, P, kb0 + ks, bb0 + 256, vE0 + vs, vO0 + vs);
      bar();
      vs = ks; ks = (ks == 2 * SLOT ? 0 : ks + SLOT);
    }
    p1_round<false, true, true>(s, accA, accB, P, kb0, bb0, vE0 + SLOT, vO0 + SLOT);
#pragma unroll
    for (int i = 0; i < NTAIL; ++i) s.acc2[6 - NTAIL + i] = mfma(asbf(s.pa1), s.vt[i], s.acc2[6 - NTAIL + i]);
    __builtin_amdgcn_s_setprio(0);
    TS(2);
    bar();
    const unsigned myreg = L0 + ((ci >> 1) == 0 ? K2 : (ci >> 1) == 1 ? K0 : V2) + (ci & 1) * TILE;
    const int cp = sg == 0 ? ci + 3 : ci - 3;
    const unsigned otreg = L0 + ((cp >> 1) == 0 ? K2 : (cp >> 1) == 1 ? K0 : V2) + (cp & 1) * TILE;
#pragma unroll
    for (int t = 0; t < 12; ++t) Q[t] = lds_rd16(myreg + (swz ^ (32 * (t & 3))) + 128 * (t >> 2));
    wait_lgkm0();
    f32x16 keep[3];
    if (sg == 0) {
#pragma unroll
      for (int n = 0; n < 3; ++n) {
#pragma unroll
        for (int g = 0; g < 4; ++g)
          *(LAS f32x4*)(myreg + lane * 16 + (n * 4 + g) * 1024) = f32x4{s.acc2[3 + n][4 * g], s.acc2[3 + n][4 * g + 1], s.acc2[3 + n][4 * g + 2], s.acc2[3 + n][4 * g + 3]};
        keep[n] = s.acc2[n];
      }
    } else {
#pragma unroll
      for (int n = 0; n < 3; ++n) {
#pragma unroll
        for (int g = 0; g < 4; ++g)
          *(LAS f32x4*)(myreg + lane * 16 + (n * 4 + g) * 1024) = f32x4{s.acc2[n][4 * g], s.acc2[n][4 * g + 1], s.acc2[n][4 * g + 2], s.acc2[n][4 * g + 3]};
        keep[n] = s.acc2[3 + n];
      }
    }
    {
      const float ksw = swap32_add(s.ksum);
      if (lane < 32) *(LAS float*)(L0 + KSS_OFF + (sg * 96 + mt * 32 + lane) * 4) = ksw;
    }
    bar();
    {
      const __amdgpu_buffer_rsrc_t srdW = __builtin_amdgcn_make_buffer_rsrc((void*)(ws + WS_PART + (size_t)(head * 4 + sq) * 73728), 0, 73728, 0x00020000);
#pragma unroll
      for (int n = 0; n < 3; ++n) {
        float r[16];
#pragma unroll
        for (int g = 0; g < 4; ++g) {
          f32x4 t = *(const LAS f32x4*)(otreg + lane * 16 + (n * 4 + g) * 1024);
          r[4 * g] = keep[n][4 * g] + t[0]; r[4 * g + 1] = keep[n][4 * g + 1] + t[1]; r[4 * g + 2] = keep[n][4 * g + 2] + t[2]; r[4 * g + 3] = keep[n][4 * g + 3] + t[3];
        }
#pragma unroll
        for (int t = 0; t < 2; ++t) {
          u32x4 f = {pk2(r[8 * t], r[8 * t + 1]), pk2(r[8 * t + 2], r[8 * t + 3]), pk2(r[8 * t + 4], r[8 * t + 5]), pk2(r[8 * t + 6], r[8 * t + 7])};
          __builtin_amdgcn_raw_buffer_store_b128(f, srdW, ((u * 2 + t) * 6 + 3 * sg + n) * 1024 + lane * 16, 0, 16);
        }
      }
      if (sg == 0 && lane < 32) {
        const float sm = *(const LAS float*)(L0 + KSS_OFF + (mt * 32 + lane) * 4) + *(const LAS float*)(L0 + KSS_OFF + (96 + mt * 32 + lane) * 4);
        __hip_atomic_store((GAS float*)(ws + WS_KSUM) + (head * 4 + sq) * 192 + u * 32 + lane, sm, __ATOMIC_RELAXED, __HIP_MEMORY_SCOPE_AGENT);
      }
    }
    biasq = q_bias(Q);
  }
  tag = tag * 0x9E3779B97F4A7C15ull + 0x632BE59BD9B4E019ull;
  TS(3);

  bar();
  TS(4);
  u32x4 phq[12];
  const __amdgpu_buffer_rsrc_t srdR = __builtin_amdgcn_make_buffer_rsrc((void*)(ws + WS_PART + (size_t)head * 4 * 73728), 0, 4 * 73728, 0x00020000);
  const unsigned fo = wave * 3 * 1024 + lane * 16;
  u32x4 X0[12], X1[12];
  float ks4 = 0.f;
#define LOADB(X, b) _Pragma("unroll") for (int i_ = 0; i_ < 12; ++i_) \
    X[i_] = __builtin_amdgcn_raw_buffer_load_b128(srdR, fo + (b) * 24576 + (i_ >> 2) * 1024, (i_ & 3) * 73728, 16)
  {
    f32x16 acc[2];
    bf16x8 pf[4];
#define PFRAG(i) asbf(lds_rd16(L0 + (((i) / 24) == 0 ? K1 : ((i) / 24) == 1 ? V0 : V1) + (((i) / 12) & 1) * TILE + (swz ^ (32 * ((i) & 3))) + 128 * (((i) % 12) >> 2)))
#pragma unroll
    for (int i = 0; i < 3; ++i) pf[i] = PFRAG(i);
    SB();
#pragma unroll
    for (int u = 0; u <= 6; ++u) {
      float e[16];
#pragma unroll
      for (int t = 0; t < 12; ++t) {
        if (u < 6) {
          const int i = u * 12 + t;
          if (t == 0) {
            f32x16 z;
#pragma unroll
            for (int j = 0; j < 16; ++j) z[j] = 0.f;
            acc[u & 1] = mfma(pf[i & 3], asbf(Q[t]), z);
          } else acc[u & 1] = mfma(pf[i & 3], asbf(Q[t]), acc[u & 1]);
          if (i + 3 < 72) pf[(i + 3) & 3] = PFRAG(i + 3);
        }
        if (u > 0) {
#pragma unroll
          for (int j = (t * 4) / 3; j < ((t + 1) * 4) / 3; ++j) {
            e[j] = __builtin_amdgcn_exp2f(__builtin_fmaf(acc[(u - 1) & 1][j], C1, biasq));
            if (j & 1) phq[2 * (u - 1) + (j >> 3)][(j >> 1) & 3] = pk2(e[j - 1], e[j]);
          }
        }
        SB();
      }
      if (u == 1 && mt != 3) {
        wait_vm<0>();
        unsigned old = 0;
        if (lane == 0) old = __hip_atomic_fetch_add((LAS unsigned*)(L0 + CNT_OFF), 1u, __ATOMIC_RELAXED, __HIP_MEMORY_SCOPE_WORKGROUP);
        old = __builtin_amdgcn_readfirstlane(old);
        if (old == 5u && lane == 0)
          __hip_atomic_store((GAS u64*)(ws + WS_FLAG + (head * 8 + c) * 256), tag, __ATOMIC_RELAXED, __HIP_MEMORY_SCOPE_AGENT);
        TS(5);
        SB();
      }
      if (u == POLL_AFTER) {
        TS(6);
        GAS u64* fl = (GAS u64*)(ws + WS_FLAG + (head * 8 + (lane & 7)) * 256);
        for (unsigned spins = 0; spins < 400000u; ++spins) {
          const u64 f = __hip_atomic_load(fl, __ATOMIC_RELAXED, __HIP_MEMORY_SCOPE_AGENT);
          if (__all(f == tag)) break;
          __builtin_amdgcn_s_sleep(1);
        }
        asm volatile("" ::: "memory");
        TS(7);
        if (c == 0 && wave == 0 && lane == 0) __hip_atomic_store((GAS u64*)(ws + WS_EPOCH + head * 256), tag, __ATOMIC_RELAXED, __HIP_MEMORY_SCOPE_AGENT);
        if (tid < 192) {
          GAS float* kp = (GAS float*)(ws + WS_KSUM) + head * 4 * 192 + tid;
          const float a0 = __hip_atomic_load(kp, __ATOMIC_RELAXED, __HIP_MEMORY_SCOPE_AGENT), a1 = __hip_atomic_load(kp + 192, __ATOMIC_RELAXED, __HIP_MEMORY_SCOPE_AGENT);
          const float a2 = __hip_atomic_load(kp + 384, __ATOMIC_RELAXED, __HIP_MEMORY_SCOPE_AGENT), a3 = __hip_atomic_load(kp + 576, __ATOMIC_RELAXED, __HIP_MEMORY_SCOPE_AGENT);
          ks4 = (a0 + a1) + (a2 + a3);
        }
        LOADB(X0, 0);
        SB();
      }
    }
#undef PFRAG
  }
  TS(8);
  const unsigned kvw[3] = {L0 + K2 + fo, L0 + K0 + fo, L0 + V2 + fo};
#define SUMB(X, b) _Pragma("unroll") for (int i_ = 0; i_ < 3; ++i_) { u32x4 o_; \
    _Pragma("unroll") for (int j_ = 0; j_ < 4; ++j_) { \
      const float lo_ = pin(((bflo(X[4 * i_][j_]) + bflo(X[4 * i_ + 1][j_])) + bflo(X[4 * i_ + 2][j_])) + bflo(X[4 * i_ + 3][j_])); \
      const float hi_ = pin(((bfhi(X[4 * i_][j_]) + bfhi(X[4 * i_ + 1][j_])) + bfhi(X[4 * i_ + 2][j_])) + bfhi(X[4 * i_ + 3][j_])); \
      o_[j_] = pk2(lo_, hi_); } \
    lds_wr16(kvw[b] + i_ * 1024, o_); }
  LOADB(X1, 1);
  SUMB(X0, 0);
  if (tid < 192) {
    const int m = tid;
    const int idx = (((m >> 4) * 2 + ((m >> 2) & 1)) * 8) + ((m >> 3) & 1) * 4 + (m & 3);
    *(LAS unsigned short*)(L0 + KSI_OFF + idx * 2) = (unsigned short)(pk2(ks4, 0.f) & 0xffffu);
  }
  LOADB(X0, 2);
  bar();
  TS(9);
  float rden;
  f32x16 a4[6];
  const unsigned kvr[3] = {L0 + K2 + lane * 16, L0 + K0 + lane * 16, L0 + V2 + lane * 16};
  {
    f32x16 acc;
#pragma unroll
    for (int i = 0; i < 16; ++i) acc[i] = 0.f;
#pragma unroll
    for (int t = 0; t < 12; ++t) acc = mfma(asbf(lds_rd16(L0 + KSI_OFF + t * 32 + hh * 16)), asbf(phq[t]), acc);
    rden = __builtin_amdgcn_rcpf(acc[0] + 1e-6f);
#pragma unroll
    for (int n = 0; n < 6; ++n)
#pragma unroll
      for (int i = 0; i < 16; ++i) a4[n][i] = 0.f;
  }
#define G4(b) _Pragma("unroll") for (int t_ = 0; t_ < 4; ++t_) _Pragma("unroll") for (int n_ = 0; n_ < 6; ++n_) \
    a4[n_] = mfma(asbf(lds_rd16(kvr[b] + (t_ * 6 + n_) * 1024)), asbf(phq[4 * (b) + t_]), a4[n_])
  G4(0);
  SUMB(X1, 1);
  bar();
  G4(1);
  SUMB(X0, 2);
  bar();
  TS(10);
  const unsigned stg = L0 + V0 + wave * 4096;
  const __amdgpu_buffer_rsrc_t srdO = __builtin_amdgcn_make_buffer_rsrc((void*)(out + ((size_t)head * SEQ + c * 256 + wave * 32) * ROWB), 0, 32 * ROWB, 0x00020000);
  const unsigned wadr = stg + l31 * 128 + 8 * (hh ^ ((l31 >> 3) & 1));
  const unsigned ooff = (lane >> 3) * ROWB + (((lane & 7) ^ (lane >> 3)) << 4);
#pragma unroll
  for (int np = 0; np < 3; ++np) {
#pragma unroll
    for (int t = 0; t < 4; ++t) {
      a4[2 * np] = mfma(asbf(lds_rd16(kvr[2] + (t * 6 + 2 * np) * 1024)), asbf(phq[8 + t]), a4[2 * np]);
      a4[2 * np + 1] = mfma(asbf(lds_rd16(kvr[2] + (t * 6 + 2 * np + 1) * 1024)), asbf(phq[8 + t]), a4[2 * np + 1]);
    }
    const f32x16 &a0 = a4[2 * np], &a1 = a4[2 * np + 1];
#pragma unroll
    for (int g = 0; g < 4; ++g) {
      u32x2 w0 = {pk2(pin(a0[4 * g] * rden), pin(a0[4 * g + 1] * rden)), pk2(pin(a0[4 * g + 2] * rden), pin(a0[4 * g + 3] * rden))};
      u32x2 w1 = {pk2(pin(a1[4 * g] * rden), pin(a1[4 * g + 1] * rden)), pk2(pin(a1[4 * g + 2] * rden), pin(a1[4 * g + 3] * rden))};
      *(LAS u32x2*)(wadr + ((g ^ (l31 & 7)) << 4)) = w0;
      *(LAS u32x2*)(wadr + (((4 + g) ^ (l31 & 7)) << 4)) = w1;
    }
#pragma unroll
    for (int i = 0; i < 4; ++i) {
      u32x4 r = lds_rd16(stg + i * 1024 + lane * 16);
      if (i & 1) r = u32x4{r[2], r[3], r[0], r[1]};
      __builtin_amdgcn_raw_buffer_store_b128(r, srdO, ooff + i * 8 * ROWB + np * 128, 0, OUT_POL);
    }
  }
}

extern "C" void kernel_launch(void* const* d_in, const int* in_sizes, int n_in, void* d_out, int out_size, void* d_ws, size_t ws_size,
                              hipStream_t stream) {
  (void)in_sizes; (void)n_in; (void)out_size; (void)ws_size;
  (void)hipFuncSetAttribute((const void*)performer_kernel, hipFuncAttributeMaxDynamicSharedMemorySize, LDS_BYTES);
  performer_kernel<<<256, 512, LDS_BYTES, stream>>>((const char*)d_in[0], (const char*)d_in[1], (const char*)d_in[2], (const char*)d_in[3],
                                                    (char*)d_out, (char*)d_ws);
}
```

```cpp
#include <hip/hip_runtime.h>
#include <stdint.h>

#define KV_POL 0
#define Q_POL 0
#define OUT_POL 2

typedef __bf16 bf16x8 __attribute__((ext_vector_type(8)));
typedef __bf16 bf16x2 __attribute__((ext_vector_type(2)));
typedef float f32x2 __attribute__((ext_vector_type(2)));
typedef float f32x4 __attribute__((ext_vector_type(4)));
typedef float f32x16 __attribute__((ext_vector_type(16)));
typedef unsigned u32x2 __attribute__((ext_vector_type(2)));
typedef unsigned u32x4 __attribute__((ext_vector_type(4)));
typedef short s16x4 __attribute__((ext_vector_type(4)));
typedef short s16x8 __attribute__((ext_vector_type(8)));
typedef unsigned long long u64;
#define LAS __attribute__((address_space(3)))
#define GAS __attribute__((address_space(1)))
#define DI __device__ __forceinline__

constexpr int NH = 32, SEQ = 2048, ROWB = 384;
constexpr int TILE = 12288;
constexpr int SLOT = 24576;
constexpr int K0 = 0, K1 = SLOT, K2 = 2 * SLOT, V0 = 3 * SLOT, V1 = 4 * SLOT, V2 = 5 * SLOT, PSP = 6 * SLOT, AUXR = PSP + TILE;
constexpr int BIAS_OFF = AUXR;
constexpr int KSS_OFF = AUXR + 512;
constexpr int KSI_OFF = AUXR + 1280;
constexpr int CNT_OFF = AUXR + 1664;
constexpr int LDS_BYTES = AUXR + 2048;

constexpr size_t WS_EPOCH = 0;
constexpr size_t WS_FLAG = 8192;
constexpr size_t WS_KSUM = 131072;
constexpr size_t WS_PART = 1048576;
constexpr size_t WS_FIN = 20971520;

constexpr float SCALE = 0.2686424829558855f;
constexpr float LOG2E = 1.4426950408889634f;
constexpr float C1 = SCALE * LOG2E;
constexpr float LOG2C = -3.7924812503605780f;

template <int POL> DI void dma16(unsigned m0v, unsigned voff, __amdgpu_buffer_rsrc_t srd, unsigned soff) {
  if constexpr (POL == 0)
    asm volatile("s_mov_b32 m0, %0\n\ts_nop 0\n\tbuffer_load_dwordx4 %1, %2, %3 offen lds" ::"s"(m0v), "v"(voff), "s"(srd), "s"(soff) : "memory");
  else if constexpr (POL == 16)
    asm volatile("s_mov_b32 m0, %0\n\ts_nop 0\n\tbuffer_load_dwordx4 %1, %2, %3 offen sc1 lds" ::"s"(m0v), "v"(voff), "s"(srd), "s"(soff) : "memory");
  else
    asm volatile("s_mov_b32 m0, %0\n\ts_nop 0\n\tbuffer_load_dwordx4 %1, %2, %3 offen nt lds" ::"s"(m0v), "v"(voff), "s"(srd), "s"(soff) : "memory");
}
template <int N> DI void wait_vm() { asm volatile("s_waitcnt vmcnt(%0)" ::"n"(N) : "memory"); }
DI void wait_lgkm0() { asm volatile("s_waitcnt lgkmcnt(0)" ::: "memory"); }
DI void bar0() { asm volatile("s_waitcnt lgkmcnt(0)\n\ts_barrier" ::: "memory"); }
DI unsigned pk2(float a, float b) { f32x2 t = {a, b}; return __builtin_bit_cast(unsigned, __builtin_convertvector(t, bf16x2)); }
DI f32x16 mfma(bf16x8 a, bf16x8 b, f32x16 c) { return __builtin_amdgcn_mfma_f32_32x32x16_bf16(a, b, c, 0, 0, 0); }
DI u32x4 lds_rd16(unsigned a) { return *(const LAS u32x4*)a; }
DI void lds_wr16(unsigned a, u32x4 v) { *(LAS u32x4*)a = v; }
DI bf16x8 asbf(u32x4 v) { return __builtin_bit_cast(bf16x8, v); }
DI s16x4 lds_tr(unsigned a) { return __builtin_amdgcn_ds_read_tr16_b64_v4i16((LAS s16x4*)a); }
DI float dot2(unsigned a, float c) { bf16x2 t = __builtin_bit_cast(bf16x2, a); return __builtin_amdgcn_fdot2_f32_bf16(t, t, c, false); }
DI float sumsq8(u32x4 v, float c) { return dot2(v[3], dot2(v[2], dot2(v[1], dot2(v[0], c)))); }
template <int CTRL> DI float dpp_add(float x) {
  return x + __builtin_bit_cast(float, __builtin_amdgcn_update_dpp(0, __builtin_bit_cast(int, x), CTRL, 0xf, 0xf, true));
}
DI float swap32_add(float x) {
  auto r = __builtin_amdgcn_permlane32_swap(__float_as_uint(x), __float_as_uint(x), false, false);
  return __uint_as_float(r[0]) + __uint_as_float(r[1]);
}
DI float bflo(unsigned x) { return __uint_as_float(x << 16); }
DI float bfhi(unsigned x) { return __uint_as_float(x & 0xffff0000u); }

#define XEXP(x) __builtin_amdgcn_exp2f(x)
#define SB() __builtin_amdgcn_sched_barrier(0)
DI float pin(float x) { asm volatile("" : "+v"(x)); return x; }

#define EPI_ST 11
#define ROT 0
#define G3R 6
#define KV4PF 4

template <int POL> DI void issue_k(unsigned lds, const unsigned (&vk)[6], __amdgpu_buffer_rsrc_t srd, unsigned soff) {
#pragma unroll
  for (int i = 0; i < 12; ++i) dma16<POL>(lds + i * 1024, vk[i % 6], srd, soff + (i / 6) * 6144);
}
template <int POL> DI void issue_v(unsigned lds, const unsigned (&vv)[3], __amdgpu_buffer_rsrc_t srd, unsigned soff) {
#pragma unroll
  for (int i = 0; i < 12; ++i) dma16<POL>(lds + i * 1024, vv[i % 3], srd, soff + (i / 3) * 3072);
}
DI void k_norms(unsigned tile, unsigned biasaddr, int lane) {
  const unsigned a = tile + (lane >> 3) * ROWB + (lane & 7) * 16;
  u32x4 x[12];
#pragma unroll
  for (int i = 0; i < 12; ++i) x[i] = lds_rd16(a + (i / 3) * 8 * ROWB + (i % 3) * 128);
  float t[12];
#pragma unroll
  for (int i = 0; i < 12; ++i) t[i] = dot2(x[i][0], 0.f);
#pragma unroll
  for (int j = 1; j < 4; ++j)
#pragma unroll
    for (int i = 0; i < 12; ++i) t[i] = dot2(x[i][j], t[i]);
  float s[4];
#pragma unroll
  for (int p = 0; p < 4; ++p) s[p] = (t[3 * p] + t[3 * p + 1]) + t[3 * p + 2];
#pragma unroll
  for (int p = 0; p < 4; ++p) s[p] = dpp_add<0xB1>(s[p]);
#pragma unroll
  for (int p = 0; p < 4; ++p) s[p] = dpp_add<0x4E>(s[p]);
#pragma unroll
  for (int p = 0; p < 4; ++p) s[p] = pin(dpp_add<0x141>(s[p]));
  const int p = lane & 3;
  float sel = p == 0 ? s[0] : p == 1 ? s[1] : p == 2 ? s[2] : s[3];
  sel = sel * (-0.5f * SCALE) + LOG2C / C1;
  if ((lane & 7) < 4) *(LAS float*)(biasaddr + (8 * p + (lane >> 3)) * 4) = sel;
}
DI float q_bias(const u32x4 (&Q)[12]) {
  float t[12];
#pragma unroll
  for (int i = 0; i < 12; ++i) t[i] = dot2(Q[i][0], 0.f);
#pragma unroll
  for (int j = 1; j < 4; ++j)
#pragma unroll
    for (int i = 0; i < 12; ++i) t[i] = dot2(Q[i][j], t[i]);
  const float s = ((t[0] + t[1]) + (t[2] + t[3])) + ((t[4] + t[5]) + (t[6] + t[7])) + ((t[8] + t[9]) + (t[10] + t[11]));
  return pin(swap32_add(s) * (-0.5f * SCALE * C1) + LOG2C);
}

DI bf16x8 v_frag(unsigned vE, unsigned vO, int i) {
  const int t = i / 6, n = i % 6;
  const unsigned base = ((n & 1) ? vO : vE) + t * 16 * ROWB + 64 * n;
  s16x4 lo = lds_tr(base), hi = lds_tr(base + 8 * ROWB);
  return __builtin_bit_cast(bf16x8, __builtin_shufflevector(lo, hi, 0, 1, 2, 3, 4, 5, 6, 7));
}
#define NTAIL 3
#define VGAP 3
#define KPF 3
struct P1 {
  f32x16 acc2[6];
  bf16x8 vt[NTAIL];
  u32x4 pa1;
  float ksum;
};
template <bool G1, bool EX, bool TAIL>
DI void p1_round(P1& s, f32x16& cur, const f32x16& prev, const bf16x8 (&P)[12], unsigned kb, unsigned biasaddr, unsigned vE, unsigned vO) {
  constexpr int NIN = 12 - NTAIL;
  constexpr int VOFF = 12 + NIN - 12 - VGAP;
  bf16x8 kf[12], vf[12];
  if constexpr (G1) {
#pragma unroll
    for (int t = 0; t < 1; ++t) kf[t] = asbf(lds_rd16((kb ^ (32 * (t & 3))) + 128 * (t >> 2)));
#pragma unroll
    for (int g = 0; g < 4; ++g) {
      f32x4 b = *(const LAS f32x4*)(biasaddr + g * 32);
      cur[4 * g] = b[0]; cur[4 * g + 1] = b[1]; cur[4 * g + 2] = b[2]; cur[4 * g + 3] = b[3];
    }
#pragma unroll
    for (int t = 1; t < KPF; ++t) kf[t] = asbf(lds_rd16((kb ^ (32 * (t & 3))) + 128 * (t >> 2)));
  }
  SB();
  if constexpr (TAIL) {
#pragma unroll
    for (int i = 0; i < NTAIL; ++i) { s.acc2[6 - NTAIL + i] = mfma(asbf(s.pa1), s.vt[i], s.acc2[6 - NTAIL + i]); SB(); }
  }
  float e[16];
  u32x4 pa0, pa1;
#define EXP1(j) do { e[j] = XEXP(prev[j] * C1); s.ksum += e[j]; \
    if ((j) & 1) { const unsigned w_ = pk2(e[(j) - 1], e[j]); if ((j) < 8) pa0[((j) >> 1) & 3] = w_; else pa1[((j) >> 1) & 3] = w_; } } while (0)
#pragma unroll
  for (int t = 0; t < 12; ++t) {
    if constexpr (G1) {
      cur = mfma(kf[t], P[t], cur);
      if (t + KPF < 12) kf[t + KPF] = asbf(lds_rd16((kb ^ (32 * ((t + KPF) & 3))) + 128 * ((t + KPF) >> 2)));
    }
    if constexpr (EX) {
      if (t >= VOFF) { if (t - VOFF < NIN) vf[t - VOFF] = v_frag(vE, vO, t - VOFF); else s.vt[t - VOFF - NIN] = v_frag(vE, vO, t - VOFF); }
      EXP1(t);
    }
    SB();
  }
  if constexpr (EX) {
#pragma unroll
    for (int i = 0; i < NIN; ++i) {
      s.acc2[i % 6] = mfma(asbf(i < 6 ? pa0 : pa1), vf[i], s.acc2[i % 6]);
      if (12 + i - VOFF < 12) { const int j = 12 + i - VOFF; if (j < NIN) vf[j] = v_frag(vE, vO, j); else s.vt[j - NIN] = v_frag(vE, vO, j); }
      if (i < 4) EXP1(12 + i);
      SB();
    }
    s.pa1 = pa1;
  }
#undef EXP1
}

__global__ __launch_bounds__(512, 2) void performer_kernel(const char* __restrict__ q, const char* __restrict__ k, const char* __restrict__ v,
                                                           const char* __restrict__ proj, char* __restrict__ out, char* ws) {
  extern __shared__ __attribute__((aligned(16))) char smem[];
  const unsigned L0 = (unsigned)(uintptr_t)(LAS char*)smem;
  const int tid = threadIdx.x, lane = tid & 63;
  const int wave = __builtin_amdgcn_readfirstlane(tid >> 6);
  const int bid = blockIdx.x;
  const int head = (bid & 7) + 8 * (bid >> 6), c = (bid >> 3) & 7;
  const int sq = c >> 1, mh = c & 1;
  const int l31 = lane & 31, hh = lane >> 5;
  const int sg = wave >> 2, mt = wave & 3;
#define bar() bar0()

  u32x4 Q[12];
  u64 tag;
  const char* ep = ws + WS_EPOCH + head * 256;
  float biasq;
  const unsigned swz = l31 * ROWB + ((hh ^ ((l31 >> 1) & 7)) << 4);

  if (mt == 3) {
    const __amdgpu_buffer_rsrc_t srdK = __builtin_amdgcn_make_buffer_rsrc((void*)(k + ((size_t)head * SEQ + sq * 512) * ROWB), 0, 512 * ROWB, 0x00020000);
    const __amdgpu_buffer_rsrc_t srdV = __builtin_amdgcn_make_buffer_rsrc((void*)(v + ((size_t)head * SEQ + sq * 512) * ROWB), 0, 512 * ROWB, 0x00020000);
    const __amdgpu_buffer_rsrc_t srdP = __builtin_amdgcn_make_buffer_rsrc((void*)proj, 0, 192 * ROWB, 0x00020000);
    const __amdgpu_buffer_rsrc_t srdQ = __builtin_amdgcn_make_buffer_rsrc((void*)(q + ((size_t)head * SEQ + c * 256) * ROWB), 0, 256 * ROWB, 0x00020000);
    unsigned vk[6], vv[3];
#pragma unroll
    for (int b = 0; b < 6; ++b) {
      const int pos = b * 64 + lane, row = pos / 24, cs = pos % 24;
      vk[b] = row * ROWB + (((cs & ~7) | ((cs & 7) ^ ((row >> 1) & 7))) << 4);
      if (b < 3) vv[b] = row * ROWB + ((cs ^ (((row >> 1) & 1) << 2)) << 4);
    }
    const unsigned lg = L0 + sg * TILE, bl = L0 + BIAS_OFF + sg * 128;
    const unsigned so = sg * TILE;
    const int rot = mh * ROT;
#define TOFF(r) (so + (unsigned)(((r) + rot) & 7) * SLOT)
    issue_k<KV_POL>(lg + K0, vk, srdK, TOFF(0)); issue_k<KV_POL>(lg + K1, vk, srdK, TOFF(1)); issue_v<KV_POL>(lg + V0, vv, srdV, TOFF(0));
    asm volatile("s_load_dwordx2 %0, %1, 0x0\n\ts_waitcnt lgkmcnt(0)" : "=s"(tag) : "s"(ep) : "memory");
    if (wave == 3 && lane < 8) *(LAS unsigned*)(L0 + CNT_OFF + lane * 4) = 0u;
    wait_vm<24>(); k_norms(lg + K0, bl, lane); bar();
    unsigned s1 = SLOT, s2 = 2 * SLOT;
#pragma unroll 1
    for (int r = 0; r < 5; ++r) {
      issue_k<KV_POL>(lg + K0 + s2, vk, srdK, TOFF(r + 2)); issue_v<KV_POL>(lg + V0 + s1, vv, srdV, TOFF(r + 1));
      wait_vm<36>(); k_norms(lg + K0 + s1, bl + ((r + 1) & 1) * 256, lane); wait_vm<24>(); bar();
      const unsigned t = s1; s1 = s2; s2 = (t == 0 ? 2 * SLOT : t - SLOT);
    }
    issue_k<KV_POL>(lg + K0 + s2, vk, srdK, TOFF(7)); issue_v<KV_POL>(lg + V0 + s1, vv, srdV, TOFF(6));
    {
      const unsigned qoff = (wave * 32 + l31) * ROWB + hh * 16;
#pragma unroll
      for (int t = 0; t < 12; ++t) Q[t] = __builtin_amdgcn_raw_buffer_load_b128(srdQ, qoff + t * 32, 0, Q_POL);
    }
    wait_vm<48>(); k_norms(lg + K0 + s1, bl, lane); wait_vm<36>(); bar();
    const int c0 = sg, c1 = 2 + sg, c2 = 4 + sg;
    issue_v<KV_POL>(lg + V1, vv, srdV, TOFF(7));
#undef TOFF
    issue_k<Q_POL>(lg + K2, vk, srdQ, (c0 + (c0 >= 3)) * TILE);
    wait_vm<48>(); k_norms(lg + K1, bl + 256, lane); wait_vm<36>(); bar();
    issue_k<Q_POL>(lg + K0, vk, srdQ, (c1 + (c1 >= 3)) * TILE);
    issue_k<Q_POL>(lg + V2, vk, srdQ, (c2 + (c2 >= 3)) * TILE);
    wait_vm<36>(); bar();
    issue_k<0>(lg + V0, vk, srdP, sg * TILE);
    bar();
    issue_k<0>(lg + V1, vk, srdP, (2 + sg) * TILE);
    bar();
    bar();
    issue_k<0>(lg + K1, vk, srdP, (4 + sg) * TILE);
    wait_vm<0>();
    if (lane == 0) __hip_atomic_fetch_add((LAS unsigned*)(L0 + CNT_OFF + 12), 1u, __ATOMIC_RELAXED, __HIP_MEMORY_SCOPE_WORKGROUP);
    biasq = q_bias(Q);
  } else {
    const int u = 3 * mh + mt, ci = sg * 3 + mt;
    bf16x8 P[12];
    const unsigned preg = L0 + (mt == 2 ? PSP : V2 + mt * TILE);
    {
      const __amdgpu_buffer_rsrc_t srdP = __builtin_amdgcn_make_buffer_rsrc((void*)proj, 0, 192 * ROWB, 0x00020000);
#pragma unroll
      for (int b = 0; b < 6; ++b) {
        const int pos = b * 64 + lane, row = pos / 24, cs = pos % 24;
        const unsigned vo = row * ROWB + (((cs & ~7) | ((cs & 7) ^ ((row >> 1) & 7))) << 4);
        dma16<0>(preg + sg * 6144 + b * 1024, vo, srdP, u * TILE + sg * 6144);
      }
    }
    asm volatile("s_load_dwordx2 %0, %1, 0x0\n\ts_waitcnt lgkmcnt(0)" : "=s"(tag) : "s"(ep) : "memory");
    P1 s;
#pragma unroll
    for (int n = 0; n < 6; ++n)
#pragma unroll
      for (int i = 0; i < 16; ++i) s.acc2[n][i] = 0.f;
    s.ksum = 0.f;
    const unsigned kb0 = L0 + K0 + sg * TILE + swz;
    const int qq = (lane & 15) >> 2, pp4 = lane & 3, x = (qq >> 1) & 1;
    const unsigned vb0 = L0 + V0 + sg * TILE + (4 * hh + qq) * ROWB + 16 * (2 * ((lane >> 4) & 1) + (pp4 >> 1)) + 8 * (pp4 & 1);
    const unsigned vE0 = vb0 + 64 * x, vO0 = vb0 - 64 * x;
    const unsigned bb0 = L0 + BIAS_OFF + sg * 128 + hh * 16;
    if (sg == 0) __builtin_amdgcn_s_setprio(1);
    f32x16 accA, accB;
    wait_vm<0>();
    bar();
#pragma unroll
    for (int t = 0; t < 12; ++t) P[t] = asbf(lds_rd16(preg + (swz ^ (32 * (t & 3))) + 128 * (t >> 2)));
    p1_round<true, false, false>(s, accA, accA, P, kb0, bb0, vE0, vO0);
    bar();
    p1_round<true, true, false>(s, accB, accA, P, kb0 + SLOT, bb0 + 256, vE0, vO0);
    bar();
    unsigned ks = 2 * SLOT, vs = SLOT;
#pragma unroll 1
    for (int r = 2; r < 8; r += 2) {
      p1_round<true, true, true>(s, accA, accB, P, kb0 + ks, bb0, vE0 + vs, vO0 + vs);
      bar();
      vs = ks; ks = (ks == 2 * SLOT ? 0 : ks + SLOT);
      p1_round<true, true, true>(s, accB, accA, P, kb0 + ks, bb0 + 256, vE0 + vs, vO0 + vs);
      bar();
      vs = ks; ks = (ks == 2 * SLOT ? 0 : ks + SLOT);
    }
    p1_round<false, true, true>(s, accA, accB, P, kb0, bb0, vE0 + SLOT, vO0 + SLOT);
#pragma unroll
    for (int i = 0; i < NTAIL; ++i) s.acc2[6 - NTAIL + i] = mfma(asbf(s.pa1), s.vt[i], s.acc2[6 - NTAIL + i]);
    __builtin_amdgcn_s_setprio(0);
    bar();
    {
      const int cp = sg == 0 ? ci + 3 : ci - 3;
      const unsigned myreg = L0 + (ci < 2 ? PSP + ci * 6144 : K1 + (ci - 2) * 6144) + lane * 16;
      const unsigned otreg = L0 + (cp < 2 ? PSP + cp * 6144 : K1 + (cp - 2) * 6144) + lane * 16;
      f32x16 keep[3];
#define PACK8(r, t) u32x4{pk2(r[8 * (t)], r[8 * (t) + 1]), pk2(r[8 * (t) + 2], r[8 * (t) + 3]), pk2(r[8 * (t) + 4], r[8 * (t) + 5]), pk2(r[8 * (t) + 6], r[8 * (t) + 7])}
      if (sg == 0) {
#pragma unroll
        for (int n = 0; n < 3; ++n) {
          lds_wr16(myreg + (2 * n) * 1024, PACK8(s.acc2[3 + n], 0)); lds_wr16(myreg + (2 * n + 1) * 1024, PACK8(s.acc2[3 + n], 1));
          keep[n] = s.acc2[n];
        }
      } else {
#pragma unroll
        for (int n = 0; n < 3; ++n) {
          lds_wr16(myreg + (2 * n) * 1024, PACK8(s.acc2[n], 0)); lds_wr16(myreg + (2 * n + 1) * 1024, PACK8(s.acc2[n], 1));
          keep[n] = s.acc2[3 + n];
        }
      }
      const float ksw = swap32_add(s.ksum);
      if (lane < 32)
        __hip_atomic_store((GAS float*)(ws + WS_KSUM) + (head * 8 + sq * 2 + sg) * 192 + u * 32 + lane, ksw, __ATOMIC_RELAXED, __HIP_MEMORY_SCOPE_AGENT);
      bar();
      u32x4 ot[6];
#pragma unroll
      for (int i = 0; i < 6; ++i) ot[i] = lds_rd16(otreg + i * 1024);
      bar();
      const __amdgpu_buffer_rsrc_t srdW = __builtin_amdgcn_make_buffer_rsrc((void*)(ws + WS_PART + (size_t)(head * 4 + sq) * 73728), 0, 73728, 0x00020000);
#pragma unroll
      for (int n = 0; n < 3; ++n)
#pragma unroll
        for (int t = 0; t < 2; ++t) {
          u32x4 f;
#pragma unroll
          for (int j = 0; j < 4; ++j)
            f[j] = pk2(pin(keep[n][8 * t + 2 * j] + bflo(ot[2 * n + t][j])), pin(keep[n][8 * t + 2 * j + 1] + bfhi(ot[2 * n + t][j])));
          __builtin_amdgcn_raw_buffer_store_b128(f, srdW, ((u * 2 + t) * 6 + 3 * sg + n) * 1024 + lane * 16, 0, 16);
        }
#undef PACK8
    }
  }
  tag = tag * 0x9E3779B97F4A7C15ull + 0x632BE59BD9B4E019ull;
  if (mt != 3) {
    wait_vm<0>();
    unsigned old = 0;
    if (lane == 0) old = __hip_atomic_fetch_add((LAS unsigned*)(L0 + CNT_OFF), 1u, __ATOMIC_RELAXED, __HIP_MEMORY_SCOPE_WORKGROUP);
    old = __builtin_amdgcn_readfirstlane(old);
    if (old == 5u && lane == 0)
      __hip_atomic_store((GAS u64*)(ws + WS_FLAG + (head * 8 + c) * 256), tag, __ATOMIC_RELAXED, __HIP_MEMORY_SCOPE_AGENT);
  }

  u32x4 phq[12];
  const __amdgpu_buffer_rsrc_t srdR = __builtin_amdgcn_make_buffer_rsrc((void*)(ws + WS_PART + (size_t)head * 4 * 73728), 0, 4 * 73728, 0x00020000);
  const __amdgpu_buffer_rsrc_t srdF = __builtin_amdgcn_make_buffer_rsrc((void*)(ws + WS_FIN + (size_t)head * 73728), 0, 73728, 0x00020000);
  GAS u64* const myflag = (GAS u64*)(ws + WS_FLAG + (head * 8 + c) * 256);
  GAS u64* const fl = (GAS u64*)(ws + WS_FLAG + (head * 8 + (lane & 7)) * 256);
  if (wave < 3) {
    GAS u64* const fa = (GAS u64*)(ws + WS_FLAG + (head * 8 + 2 * (lane & 3) + (c >> 2)) * 256);
    for (unsigned spins = 0; spins < 400000u; ++spins) {
      const u64 f = __hip_atomic_load(fa, __ATOMIC_RELAXED, __HIP_MEMORY_SCOPE_AGENT);
      if (__all(f == tag)) break;
      __builtin_amdgcn_s_sleep(1);
    }
    asm volatile("" ::: "memory");
    u32x4 X[3][4];
#pragma unroll
    for (int h = 0; h < 3; ++h)
#pragma unroll
      for (int j = 0; j < 4; ++j) X[h][j] = __builtin_amdgcn_raw_buffer_load_b128(srdR, (c * 9 + mt * 3 + h) * 1024 + lane * 16, j * 73728, 16);
#pragma unroll
    for (int h = 0; h < 3; ++h) {
      u32x4 o;
#pragma unroll
      for (int j = 0; j < 4; ++j) {
        const float lo = (bflo(X[h][0][j]) + bflo(X[h][1][j])) + (bflo(X[h][2][j]) + bflo(X[h][3][j]));
        const float hi = (bfhi(X[h][0][j]) + bfhi(X[h][1][j])) + (bfhi(X[h][2][j]) + bfhi(X[h][3][j]));
        o[j] = pk2(lo, hi);
      }
      __builtin_amdgcn_raw_buffer_store_b128(o, srdF, (c * 9 + mt * 3 + h) * 1024 + lane * 16, 0, 16);
    }
    wait_vm<0>();
    unsigned old = 0;
    if (lane == 0) old = __hip_atomic_fetch_add((LAS unsigned*)(L0 + CNT_OFF + 4), 1u, __ATOMIC_RELAXED, __HIP_MEMORY_SCOPE_WORKGROUP);
    old = __builtin_amdgcn_readfirstlane(old);
    if (old == 2u && lane == 0) __hip_atomic_store(myflag + 16, tag, __ATOMIC_RELAXED, __HIP_MEMORY_SCOPE_AGENT);
  }
  for (unsigned spins = 0; spins < 4000000u; ++spins) {
    if (*(volatile LAS unsigned*)(L0 + CNT_OFF + 12) >= 2u) break;
    __builtin_amdgcn_s_sleep(1);
  }
  asm volatile("" ::: "memory");
  if (mt != 3) {
    const int ci = sg * 3 + mt;
    const unsigned qreg = L0 + ((ci >> 1) == 0 ? K2 : (ci >> 1) == 1 ? K0 : V2) + (ci & 1) * TILE;
#pragma unroll
    for (int t = 0; t < 12; ++t) Q[t] = lds_rd16(qreg + (swz ^ (32 * (t & 3))) + 128 * (t >> 2));
    biasq = q_bias(Q);
  }
  wait_lgkm0();
  if (lane == 0) __hip_atomic_fetch_add((LAS unsigned*)(L0 + CNT_OFF + 8), 1u, __ATOMIC_RELAXED, __HIP_MEMORY_SCOPE_WORKGROUP);
  {
    f32x16 acc[2][2];
    bf16x8 pf[G3R];
#define PFRAG0(i) asbf(lds_rd16(L0 + (((i) / 24) == 0 ? V0 : ((i) / 24) == 1 ? V1 : K1) + (((i) / 12) & 1) * TILE + (swz ^ (32 * ((i) & 3))) + 128 * (((i) % 12) >> 2)))
#define PFRAG(n) PFRAG0((2 * ((n) / 24) + ((n) & 1)) * 12 + (((n) % 24) >> 1))
#pragma unroll
    for (int i = 0; i < G3R - 1; ++i) pf[i] = PFRAG(i);
    SB();
#pragma unroll
    for (int p = 0; p <= 3; ++p) {
      float e[32];
#pragma unroll
      for (int sl = 0; sl < 24; ++sl) {
        if (p < 3) {
          const int n = p * 24 + sl, t = sl >> 1, h = sl & 1;
          if (t == 0) {
            f32x16 z;
#pragma unroll
            for (int j = 0; j < 16; ++j) z[j] = 0.f;
            acc[p & 1][h] = mfma(pf[n % G3R], asbf(Q[t]), z);
          } else acc[p & 1][h] = mfma(pf[n % G3R], asbf(Q[t]), acc[p & 1][h]);
          if (n + G3R - 1 < 72) pf[(n + G3R - 1) % G3R] = PFRAG(n + G3R - 1);
        }
        if (p > 0) {
#pragma unroll
          for (int j = (sl * 4) / 3; j < ((sl + 1) * 4) / 3; ++j) {
            e[j] = __builtin_amdgcn_exp2f(__builtin_fmaf(acc[(p - 1) & 1][j >> 4][j & 15], C1, biasq));
            if (j & 1) phq[4 * (p - 1) + (j >> 3)][(j >> 1) & 3] = pk2(e[j - 1], e[j]);
          }
        }
        SB();
      }
    }
#undef PFRAG0
#undef PFRAG
#pragma unroll
    for (int i = 0; i < 12; ++i) asm volatile("" : "+v"(phq[i]));
  }
  float rden = 0.f;
  auto den = [&]() {
    for (unsigned spins = 0; spins < 4000000u; ++spins) {
      if (*(volatile LAS unsigned*)(L0 + CNT_OFF + 16) >= 1u) break;
      __builtin_amdgcn_s_sleep(1);
    }
    asm volatile("" ::: "memory");
    f32x16 ad;
#pragma unroll
    for (int j = 0; j < 16; ++j) ad[j] = 0.f;
#pragma unroll
    for (int t = 0; t < 12; ++t) ad = mfma(asbf(lds_rd16(L0 + KSI_OFF + t * 32 + hh * 16)), asbf(phq[t]), ad);
    rden = __builtin_amdgcn_rcpf(ad[0] + 1e-6f);
    asm volatile("" : "+v"(rden));
  };
  if (wave == 3) {
    for (unsigned spins = 0; spins < 400000u; ++spins) {
      const u64 f = __hip_atomic_load(fl, __ATOMIC_RELAXED, __HIP_MEMORY_SCOPE_AGENT);
      if (__all(f == tag)) break;
      __builtin_amdgcn_s_sleep(1);
    }
    asm volatile("" ::: "memory");
    float ka[3][8];
#pragma unroll
    for (int i = 0; i < 3; ++i) {
      GAS float* kp = (GAS float*)(ws + WS_KSUM) + head * 8 * 192 + i * 64 + lane;
#pragma unroll
      for (int j = 0; j < 8; ++j) ka[i][j] = __hip_atomic_load(kp + j * 192, __ATOMIC_RELAXED, __HIP_MEMORY_SCOPE_AGENT);
    }
#pragma unroll
    for (int i = 0; i < 3; ++i) {
      const int km = i * 64 + lane;
      const float ks = ((ka[i][0] + ka[i][1]) + (ka[i][2] + ka[i][3])) + ((ka[i][4] + ka[i][5]) + (ka[i][6] + ka[i][7]));
      const int idx = (((km >> 4) * 2 + ((km >> 2) & 1)) * 8) + ((km >> 3) & 1) * 4 + (km & 3);
      *(LAS unsigned short*)(L0 + KSI_OFF + idx * 2) = (unsigned short)(pk2(ks, 0.f) & 0xffffu);
    }
    wait_lgkm0();
    if (lane == 0) __hip_atomic_fetch_add((LAS unsigned*)(L0 + CNT_OFF + 16), 1u, __ATOMIC_RELAXED, __HIP_MEMORY_SCOPE_WORKGROUP);
  }
  if (sg == 0) den();
  if (sg == 1) {
    for (unsigned spins = 0; spins < 400000u; ++spins) {
      const u64 f = __hip_atomic_load(fl + 16, __ATOMIC_RELAXED, __HIP_MEMORY_SCOPE_AGENT);
      if (__all(f == tag)) break;
      __builtin_amdgcn_s_sleep(1);
    }
    asm volatile("" ::: "memory");
    for (unsigned spins = 0; spins < 4000000u; ++spins) {
      if (*(volatile LAS unsigned*)(L0 + CNT_OFF + 8) >= 8u) break;
      __builtin_amdgcn_s_sleep(1);
    }
    asm volatile("" ::: "memory");
    if (c == 0 && wave == 4 && lane == 0) __hip_atomic_store((GAS u64*)(ws + WS_EPOCH + head * 256), tag, __ATOMIC_RELAXED, __HIP_MEMORY_SCOPE_AGENT);
    const unsigned l16 = lane * 16;
#pragma unroll
    for (int j = 0; j < 18; ++j) {
      const int f = (mt * 3 + ((j % 6) >> 1)) * 6 + 2 * (j / 6) + (j & 1), b = f / 24;
      dma16<16>(L0 + (b == 0 ? K2 : b == 1 ? K0 : V2) + (f - b * 24) * 1024, l16, srdF, f * 1024);
    }
    den();
    wait_vm<12>();
  }
  bar();
  if (sg == 1) {
    wait_vm<0>();
    if (lane == 0) __hip_atomic_fetch_add((LAS unsigned*)(L0 + CNT_OFF + 20), 1u, __ATOMIC_RELAXED, __HIP_MEMORY_SCOPE_WORKGROUP);
  }
  const unsigned kvr[3] = {L0 + K2 + lane * 16, L0 + K0 + lane * 16, L0 + V2 + lane * 16};
  const unsigned stg = L0 + V0 + wave * 4096;
  const __amdgpu_buffer_rsrc_t srdO = __builtin_amdgcn_make_buffer_rsrc((void*)(out + ((size_t)head * SEQ + c * 256 + wave * 32) * ROWB), 0, 32 * ROWB, 0x00020000);
  const unsigned wadr = stg + l31 * 128 + 8 * (hh ^ ((l31 >> 3) & 1));
  const unsigned ooff = (lane >> 3) * ROWB + (((lane & 7) ^ (lane >> 3)) << 4);
  {
    constexpr int PF = KV4PF;
    f32x16 a4[2][2];
    bf16x8 fr[PF];
    u32x4 rr[4];
#define KVFRAG(i) asbf(lds_rd16(kvr[(((i) % 24) >> 1) >> 2] + (((((i) % 24) >> 1) & 3) * 6 + 2 * ((i) / 24) + ((i) & 1)) * 1024))
#pragma unroll
    for (int i = 0; i < PF; ++i) fr[i] = KVFRAG(i);
    SB();
#pragma unroll
    for (int np = 0; np <= 3; ++np) {
#pragma unroll
      for (int t = 0; t < 12; ++t) {
#pragma unroll
        for (int h = 0; h < 2; ++h) {
          if (np < 3) {
            const int i = np * 24 + t * 2 + h;
            if (t == 0) {
              f32x16 z;
#pragma unroll
              for (int j = 0; j < 16; ++j) z[j] = 0.f;
              a4[np & 1][h] = mfma(fr[i % PF], asbf(phq[t]), z);
            } else a4[np & 1][h] = mfma(fr[i % PF], asbf(phq[t]), a4[np & 1][h]);
            if (i + PF == 24) {
              for (unsigned spins = 0; spins < 4000000u; ++spins) {
                if (*(volatile LAS unsigned*)(L0 + CNT_OFF + 20) >= 4u) break;
                __builtin_amdgcn_s_sleep(1);
              }
              asm volatile("" ::: "memory");
            }
            if (i + PF < 72) fr[i % PF] = KVFRAG(i + PF);
          }
          if (np > 0) {
            const int sl = t * 2 + h;
            if (sl < 8) {
              const f32x16& a = a4[(np - 1) & 1][sl & 1];
              const int g = sl >> 1;
              const unsigned e0 = pk2(pin(a[4 * g] * rden), pin(a[4 * g + 1] * rden)), e1 = pk2(pin(a[4 * g + 2] * rden), pin(a[4 * g + 3] * rden));
              *(LAS u32x2*)(wadr + (((4 * (sl & 1) + g) ^ (l31 & 7)) << 4)) = u32x2{e0, e1};
            } else if (sl < 10) {
              rr[2 * (sl - 8)] = lds_rd16(stg + (2 * (sl - 8)) * 1024 + lane * 16);
              rr[2 * (sl - 8) + 1] = lds_rd16(stg + (2 * (sl - 8) + 1) * 1024 + lane * 16);
            } else if (sl >= EPI_ST && sl < EPI_ST + 4) {
              const int i = sl - EPI_ST;
              u32x4 r = rr[i];
              if (i & 1) r = u32x4{r[2], r[3], r[0], r[1]};
              __builtin_amdgcn_raw_buffer_store_b128(r, srdO, ooff + i * 8 * ROWB + (np - 1) * 128, 0, OUT_POL);
            }
          }
          SB();
        }
      }
    }
#undef KVFRAG
  }
}

extern "C" void kernel_launch(void* const* d_in, const int* in_sizes, int n_in, void* d_out, int out_size, void* d_ws, size_t ws_size,
                              hipStream_t stream) {
  (void)in_sizes; (void)n_in; (void)out_size; (void)ws_size;
  (void)hipFuncSetAttribute((const void*)performer_kernel, hipFuncAttributeMaxDynamicSharedMemorySize, LDS_BYTES);
  performer_kernel<<<256, 512, LDS_BYTES, stream>>>((const char*)d_in[0], (const char*)d_in[1], (const char*)d_in[2], (const char*)d_in[3],
                                                    (char*)d_out, (char*)d_ws);
}
```
